```python
import torch

def _softmax(x, axis=-1):
    return torch.softmax(x, dim=axis)
def _gelu(x):
    return torch.nn.functional.gelu(x, approximate="tanh")
def _silu(x):
    return torch.nn.functional.silu(x)
def _sigmoid(x):
    return torch.sigmoid(x)
def _layernorm(x, axis=-1, eps=1e-5):
    m = x.mean(dim=axis, keepdim=True)
    v = x.var(dim=axis, keepdim=True, unbiased=False)
    return (x - m) / torch.sqrt(v + eps)
def _scatter_add(tgt, idx, src, axis=0):
    return tgt.clone().index_add_(axis, idx, src)
def _segment_sum(src, seg, n):
    out = torch.zeros((n,) + src.shape[1:], dtype=src.dtype)
    return out.index_add_(0, seg, src)
def _pad(x, pw):
    flat = tuple(v for pair in reversed(pw) for v in pair)
    return torch.nn.functional.pad(x, flat)

kernel_name = 'concat_heads__mha_block_cached__mha_block_cached__decode'

def setup_inputs():

    _w = (lambda a: (lambda t: t.to(torch.bfloat16) if t.is_floating_point() else t)(torch.as_tensor(a)))
    def _A_setup():
        B, Sq, D = 2, 1, 512
        H, Sk, Dh = 32, 8192, 128
        return {
            "x": _w((lambda *s: torch.randn(*s, dtype=torch.bfloat16) * 0.8)(B, Sq, D)),
            "k_cache": _w((lambda *s: torch.randn(*s, dtype=torch.bfloat16) * 0.8)(B, H, Sk, Dh)),
            "v_cache": _w((lambda *s: torch.randn(*s, dtype=torch.bfloat16) * 0.05)(B, H, Sk, Dh)),
            "wq": _w((lambda *s: torch.randn(*s, dtype=torch.bfloat16) * 0.8)(D, H * Dh) * (D ** -0.5)),
            "wo": _w((lambda *s: torch.randn(*s, dtype=torch.bfloat16) * 0.05)(H * Dh, D) * ((H * Dh) ** -0.5)),
        }
    _A_in = _A_setup()

    def _B_setup():
        B, Sq, D = 2, 1, 512
        H, Sk, Dh = 32, 8192, 128
        return {
            "x": _w((lambda *s: torch.randn(*s, dtype=torch.bfloat16) * 0.8)(B, Sq, D)),
            "k_cache": _w((lambda *s: torch.randn(*s, dtype=torch.bfloat16) * 0.8)(B, H, Sk, Dh)),
            "v_cache": _w((lambda *s: torch.randn(*s, dtype=torch.bfloat16) * 0.05)(B, H, Sk, Dh)),
            "wq": _w((lambda *s: torch.randn(*s, dtype=torch.bfloat16) * 0.8)(D, H * Dh) * (D ** -0.5)),
            "wo": _w((lambda *s: torch.randn(*s, dtype=torch.bfloat16) * 0.05)(H * Dh, D) * ((H * Dh) ** -0.5)),
        }
    _B_in = _B_setup()

    return {
        "A_x": _A_in["x"],
        "A_k_cache": _A_in["k_cache"],
        "A_v_cache": _A_in["v_cache"],
        "A_wq": _A_in["wq"],
        "A_wo": _A_in["wo"],
        "B_x": _B_in["x"],
        "B_k_cache": _B_in["k_cache"],
        "B_v_cache": _B_in["v_cache"],
        "B_wq": _B_in["wq"],
        "B_wo": _B_in["wo"],
    }

def reference(A_x, A_k_cache, A_v_cache, A_wq, A_wo, B_x, B_k_cache, B_v_cache, B_wq, B_wo):
    def _A_ref(x, k_cache, v_cache, wq, wo):
        B, Sq, D = x.shape
        H, Dh = 32, 128
        q = (lambda x, *d: x.permute(*d))((x @ wq).reshape(B, Sq, H, Dh), 0, 2, 1, 3)
        scores = torch.einsum("bhqd,bhkd->bhqk", q, k_cache) * (Dh ** -0.5)
        p = _softmax(scores, axis=-1)
        o = torch.einsum("bhqk,bhkd->bhqd", p, v_cache)
        return (lambda x, *d: x.permute(*d))(o, 0, 2, 1, 3).reshape(B, Sq, H * Dh) @ wo

    def _B_ref(x, k_cache, v_cache, wq, wo):
        B, Sq, D = x.shape
        H, Dh = 32, 128
        q = (lambda x, *d: x.permute(*d))((x @ wq).reshape(B, Sq, H, Dh), 0, 2, 1, 3)
        scores = torch.einsum("bhqd,bhkd->bhqk", q, k_cache) * (Dh ** -0.5)
        p = _softmax(scores, axis=-1)
        o = torch.einsum("bhqk,bhkd->bhqd", p, v_cache)
        return (lambda x, *d: x.permute(*d))(o, 0, 2, 1, 3).reshape(B, Sq, H * Dh) @ wo

    a = _A_ref(A_x, A_k_cache, A_v_cache, A_wq, A_wo)
    b = _B_ref(B_x, B_k_cache, B_v_cache, B_wq, B_wo)
    return torch.concatenate([a, b], axis=-1)
```

```cpp
#define QD 110
#define TEND 7210
#include <hip/hip_runtime.h>
#include <hip/hip_bf16.h>
#include <stdint.h>
#include <type_traits>

typedef unsigned u32x4 __attribute__((ext_vector_type(4)));
typedef unsigned u32x2 __attribute__((ext_vector_type(2)));
typedef float f32x4 __attribute__((ext_vector_type(4)));
typedef float f32x2 __attribute__((ext_vector_type(2)));
typedef __bf16 bf16x2 __attribute__((ext_vector_type(2)));
typedef __attribute__((address_space(1))) unsigned long long gu64;

#define PACE 7300
#define XS 470
#define DEPTH 12
#define G1 24
#define G2 16
#define GA 30
#define OWN0 0
#define OWN8 3
#define EP_LAST 1
#define WGANCH 1
#define DP1 0
#define DP2 0
#define DP3 0
#define EARLY 8
#define JIT 0
#define JMUL 5
#define EGAP 0
#define SWARM 0
#define GA1 0
#define FASTT 1
#define PG1 1
#define PG2 1
#define CRED1 0
#define JUMP 0
#define KEEP 20
#define REGAIN 0
#define PTOUCH 1
#define XLO 120
#define XHI 200
#define TB_LATE 1
#define PT2 0
#define ITH 5
#define ISTEP 360
#define ITHO 7
#define IPROP 1
#define LEAK 5
#define WSYNC 1
#define ISTEPO 240
#define WQT 0
#define XDEAD 60
#define XNHI 0
#define PMIN 6700
#define RT 0
#define PN 6900
#define CS 120
#define CN 60
#define QA_WAIT 550
#define QGAP 12
#define NE 1
#define QFIRST 1
#define PUTX 0
#define QW 0
#define QSOON 0
#define KFIRST 1
#define QB_WAIT 2500

constexpr int NH = 32, SK = 8192, DH = 128, DM = 512;
constexpr int SW = 4;
constexpr int HEAD_B = SK * DH * 2;
constexpr int WQ_ROW = NH * DH * 2;
constexpr int NG = 8;
constexpr float SC = 0.08838834764831845f * 1.4426950408889634f;

#define STAMP(i) do {} while (0)
#define STFLUSH() do {} while (0)
struct Params {
  const uint16_t* kv[4];
  const uint16_t* x[2];
  const uint16_t* wq[2];
  const uint16_t* wo[2];
  unsigned long long* ws;
  uint16_t* out;
};
constexpr int EH_OFF = 0, ER_OFF = 32 * 32;
constexpr int QX_OFF = 2048;
constexpr int OX_OFF = QX_OFF + 2 * 32 * 2 * 8 * 128;
constexpr int OX_STR = 144;
constexpr int YX_OFF = OX_OFF + 2 * 32 * 8 * OX_STR;
constexpr unsigned SPIN_CAP = 1u << 17;

__device__ __forceinline__ float dot2(unsigned a, unsigned b, float acc) {
  return __builtin_amdgcn_fdot2_f32_bf16(__builtin_bit_cast(bf16x2, a), __builtin_bit_cast(bf16x2, b), acc, false);
}
template <int CTRL> __device__ __forceinline__ float dpp(float x) {
  return __builtin_bit_cast(float, __builtin_amdgcn_mov_dpp(__builtin_bit_cast(int, x), CTRL, 0xf, 0xf, true));
}
constexpr int XOR1 = 0xB1, XOR2 = 0x4E, XOR7 = 0x141, XOR8 = 0x128;
__device__ __forceinline__ float rowsum16(float x) { x += dpp<XOR8>(x); x += dpp<XOR7>(x); x += dpp<XOR2>(x); x += dpp<XOR1>(x); return x; }
__device__ __forceinline__ float rowmax16(float x) { x = fmaxf(x, dpp<XOR8>(x)); x = fmaxf(x, dpp<XOR7>(x)); x = fmaxf(x, dpp<XOR2>(x)); x = fmaxf(x, dpp<XOR1>(x)); return x; }
__device__ __forceinline__ float tsum32(float a, float b) {
  auto t = __builtin_amdgcn_permlane32_swap(__float_as_uint(a), __float_as_uint(b), false, false);
  return __uint_as_float(t[0]) + __uint_as_float(t[1]);
}
__device__ __forceinline__ float tsum16(float a, float b) {
  auto t = __builtin_amdgcn_permlane16_swap(__float_as_uint(a), __float_as_uint(b), false, false);
  return __uint_as_float(t[0]) + __uint_as_float(t[1]);
}
__device__ __forceinline__ float xrow_sum(float x) { x = tsum16(x, x); return tsum32(x, x); }
__device__ __forceinline__ float xrow_max(float x) {
  auto s = __builtin_amdgcn_permlane16_swap(__float_as_uint(x), __float_as_uint(x), false, false);
  x = fmaxf(__uint_as_float(s[0]), __uint_as_float(s[1]));
  auto t = __builtin_amdgcn_permlane32_swap(__float_as_uint(x), __float_as_uint(x), false, false);
  return fmaxf(__uint_as_float(t[0]), __uint_as_float(t[1]));
}
__device__ __forceinline__ f32x2 rows_reduce8(const float (&a)[8]) {
  const float a0 = tsum32(a[0], a[4]), a1 = tsum32(a[1], a[5]), a2 = tsum32(a[2], a[6]), a3 = tsum32(a[3], a[7]);
  return f32x2{tsum16(a0, a2), tsum16(a1, a3)};
}
__device__ __forceinline__ float blo(unsigned u) { return __uint_as_float(u << 16); }
__device__ __forceinline__ float bhi(unsigned u) { return __uint_as_float(u & 0xffff0000u); }
__device__ __forceinline__ unsigned pack_bf16(float lo, float hi) {
  bf16x2 r; r[0] = (__bf16)lo; r[1] = (__bf16)hi; return __builtin_bit_cast(unsigned, r);
}
__device__ __forceinline__ unsigned now32() { return (unsigned)__builtin_amdgcn_s_memrealtime(); }
__device__ __forceinline__ void wg_barrier() {
  __builtin_amdgcn_sched_barrier(0);
  asm volatile("s_waitcnt lgkmcnt(0)" ::: "memory");
  __builtin_amdgcn_s_barrier();
  asm volatile("" ::: "memory");
  __builtin_amdgcn_sched_barrier(0);
}
__device__ __forceinline__ unsigned lds_addr(const void* p) { return (unsigned)(uintptr_t)(const __attribute__((address_space(3))) void*)p; }
__device__ __forceinline__ void lds_arrive(unsigned* cnt, int lane) {
  asm volatile("s_waitcnt lgkmcnt(0)\n\tds_add_u32 %0, %1" :: "v"(lds_addr(cnt)), "v"(lane == 0 ? 1u : 0u) : "memory");
}
__device__ __forceinline__ void lds_await(unsigned* cnt, unsigned n) {
  for (;;) {
    unsigned v;
    asm volatile("ds_read_b32 %0, %1\n\ts_waitcnt lgkmcnt(0)" : "=&v"(v) : "v"(lds_addr(cnt)) : "memory");
    if ((unsigned)__builtin_amdgcn_readfirstlane(v) >= n) break;
    __builtin_amdgcn_s_sleep(1);
  }
  __builtin_amdgcn_sched_barrier(0);
}
struct Tag { unsigned lo, hi; };
template <int X = 0> __device__ __forceinline__ void put_bits(gu64* g, unsigned top24, Tag t) {
  if (X) (void)__hip_atomic_exchange(g, ((unsigned long long)(top24 | t.hi) << 32) | t.lo, __ATOMIC_RELAXED, __HIP_MEMORY_SCOPE_AGENT);
  else __hip_atomic_store(g, ((unsigned long long)(top24 | t.hi) << 32) | t.lo, __ATOMIC_RELAXED, __HIP_MEMORY_SCOPE_AGENT);
}
template <int X = 0> __device__ __forceinline__ void put(gu64* g, float v, Tag t) {
  const unsigned u = __float_as_uint(v);
  unsigned a = u + 0x80u, n = u | 0x400000u;
  asm volatile("" : "+v"(a), "+v"(n));
  put_bits<X>(g, (v != v ? n : a) & 0xffffff00u, t);
}
template <int N, int GAP = 1, typename F> __device__ __forceinline__ bool sweep(F addr, Tag t, unsigned (&v)[N]) {
  for (unsigned spins = 0; spins < SPIN_CAP; ++spins) {
    unsigned long long x[N];
#pragma unroll
    for (int i = 0; i < N; ++i) x[i] = __hip_atomic_load(addr(i), __ATOMIC_RELAXED, __HIP_MEMORY_SCOPE_AGENT);
    unsigned bad = 0;
#pragma unroll
    for (int i = 0; i < N; ++i) { const unsigned hi = (unsigned)(x[i] >> 32); bad |= ((unsigned)x[i] ^ t.lo) | ((hi ^ t.hi) & 0xffu); v[i] = hi & 0xffffff00u; }
    if (__all(bad == 0)) return true;
    __builtin_amdgcn_s_sleep(GAP);
  }
  return false;
}
#define BLOAD(rs, vo, so) __builtin_amdgcn_raw_buffer_load_b128((rs), (vo), (so), 2  )
#define RSRC(ptr, bytes) __builtin_amdgcn_make_buffer_rsrc((void*)(ptr), 0, (bytes), 0x00020000)
template <int N> using IC = std::integral_constant<int, N>;
#define FOR16(X) X(0) X(1) X(2) X(3) X(4) X(5) X(6) X(7) X(8) X(9) X(10) X(11) X(12) X(13) X(14) X(15)

__global__ void __launch_bounds__(512, 2) attn_main(Params p) {
  constexpr int L_WL = 0, L_SC = L_WL + 2 * 16 * 256, L_OW = L_SC + SW * NG * 64, L_QP = L_OW + 2 * SW * DH,
                L_QB = L_QP + 4 * 2 * DH, L_ML = L_QB + 2 * 64, L_CN = L_ML + 16, L_END = L_CN + 12;
  __shared__ __attribute__((aligned(16))) float lds[L_END];
  u32x4* wl = reinterpret_cast<u32x4*>(lds + L_WL);
  float* sco = lds + L_SC;
  float* ow = lds + L_OW;
  float* qp = lds + L_QP;
  unsigned* qbs = reinterpret_cast<unsigned*>(lds + L_QB);
  float* mlw = lds + L_ML;
  unsigned* cnt = reinterpret_cast<unsigned*>(lds + L_CN);
  const int tid = threadIdx.x, lane = tid & 63, r = lane >> 4, c = lane & 15;
  const int w = __builtin_amdgcn_readfirstlane(tid >> 6);
  const int bid = blockIdx.x, j = bid >> 3;
  const int h = (bid & 7) * 4 + (j >> 3), s8 = j & 7, b = s8 >> 2, quarter = s8 & 3;

  if (w < SW) {
    __builtin_amdgcn_s_waitcnt(0x0F70);
    unsigned tbeg = now32();
    const bool slow = ((((unsigned)(uint64_t)p.wo[0] >> 7) + s8) & 7) == 3;
    if (w == 0) STAMP(0);
    const size_t kvoff = (size_t)(b * NH + h) * SK * DH;
    const int vo = lane * 16 + w * 1024 + quarter * 32768;
    const int ph = ((h & 1) << 17) | (b << 18);
    uint64_t k0 = (uint64_t)(p.kv[0] + kvoff), k1 = (uint64_t)(p.kv[1] + kvoff), k2 = (uint64_t)(p.kv[2] + kvoff), k3 = (uint64_t)(p.kv[3] + kvoff);
    asm volatile("" : "+s"(k0), "+s"(k1), "+s"(k2), "+s"(k3));
    if (TB_LATE) tbeg = now32();
    auto rdesc = [=](int n) { const uint64_t lo = (n & 16) ? k1 : k0, hi = (n & 16) ? k3 : k2; return RSRC((n & 32) ? hi : lo, n < 64 ? HEAD_B : 0); };
    auto roff = [=](int n) { return ((n & 15) << 17) ^ ph; };
    unsigned dstep = (unsigned)PACE * 64u / 128u, due = 0;
#define TR(idx) do {} while (0)
    unsigned prev = 0, due2 = 0, slip = 0;
    const bool oddx = __builtin_amdgcn_s_getreg(20 | (3 << 11)) & 1u;
    const unsigned ith = oddx ? ITHO : ITH, istep = oddx ? ISTEPO : ISTEP;
    auto pace = [&](int n) {
      if (n < 64) {
        const unsigned d = (JUMP && (int)(due2 - due) > 0) ? due2 : due;
        unsigned now = now32() << 6;
        while ((int)(now - d) < 0) { __builtin_amdgcn_s_sleep(1); now = now32() << 6; }
        if (ITH && n > 0) {
          const unsigned ib = __builtin_amdgcn_s_getreg(7 | (3 << 11)) & 15u;
          unsigned add = ib >= ith ? (IPROP ? (ib - ith + 1) * istep : istep) : 0u;
          const unsigned back = LEAK ? slip >> LEAK : 0u; slip += add - back; add -= back;
          due += add;
        }
        if (JUMP) {
          unsigned late = now - d;
          if ((int)(late - prev) > (int)(JUMP * 64u)) { due2 = now - KEEP * 64u; late = KEEP * 64u; }
          else if (d == due) due2 = due;
          prev = late;
          due2 += dstep - REGAIN;
        }
      }
      due += dstep;
    };
    u32x4 R[16], E[NE][16];
    unsigned tch = 0;
    {
      const auto rs = rdesc(0);
      auto first = [&](int i) { (i < 16 ? R[i & 15] : E[i < 16 ? 0 : (i >> 4) - 1][i & 15]) = BLOAD(rs, vo, roff(i >> 3) + (i & 7) * 4096); __builtin_amdgcn_sched_barrier(0); };
      if (tid < 11) cnt[tid] = tid == 3 ? ~0u : 0u;
#pragma unroll
      for (int i = 0; i < KFIRST; ++i) first(i);
      wg_barrier();
#pragma unroll
      for (int i = KFIRST; i < EARLY; ++i) first(i);
      if (PTOUCH && w == 0) {
        uint64_t a = ((lane & 2) ? k3 : ((lane & 1) ? k2 : k1)) + quarter * 32768 + roff(0);
        if (PTOUCH == 2 && (h & 3) == 0 && (lane & 3) == 3) a = (uint64_t)p.out + (s8 + (h & 12) * 2) * 128;
        tch = __builtin_nontemporal_load((const __attribute__((address_space(1))) unsigned*)a);
      }
      if (!QFIRST && tid < 11) cnt[tid] = tid == 3 ? ~0u : 0u;
      wg_barrier();
#pragma unroll
      for (int i = EARLY; i < EARLY + 8; ++i) first(i);
      asm volatile("" : "+v"(EARLY < 16 ? R[EARLY & 15] : E[0][EARLY & 15]));
      asm volatile("" :: "v"(tch));
      unsigned t = now32();
      if (WGANCH) {
        unsigned o = 0;
        if (lane == 0) o = __hip_atomic_fetch_min(cnt + 3, t, __ATOMIC_RELAXED, __HIP_MEMORY_SCOPE_WORKGROUP);
        t = min(t, (unsigned)__builtin_amdgcn_readfirstlane(o));
      }
      {
        constexpr int NS = 126 - EARLY / 4;
        const int ts = (int)(t - tbeg), left = max(TEND - ts, NS * PN / 128 - (slow ? CS : CN));
        dstep = (unsigned)min(max(left * 64 / NS, PMIN / 2), PACE / 2);
      }
      due = (t << 6) - (unsigned)((2 * h + b) * JMUL & 15) * (JIT * 4u);
      due2 = due;
      if (w == 0) STAMP(7);
#pragma unroll
      for (int i = EARLY + 8; i < 16 + 16 * NE; ++i) { if (i % 4 == 0) pace(0); first(i); }
    }
    u32x4 q;
    float x[16], mx, acc[8], l, pr;
    auto kdot = [&](u32x4& K, int i) {
      asm volatile("" : "+v"(K));
      float d = dot2(K[0], q[0], 0.f);
      d = dot2(K[1], q[1], d); d = dot2(K[2], q[2], d); d = dot2(K[3], q[3], d);
      asm volatile("" : "+v"(d));
      x[i] = d;
    };
    auto kfinish = [&](int g) {
#define TR_STEP(HALF, CTRL) _Pragma("unroll") for (int i = 0; i < (HALF); ++i) { \
        const float t1 = x[i] + dpp<CTRL>(x[i]), t2 = x[i + (HALF)] + dpp<CTRL>(x[i + (HALF)]); x[i] = (c & (HALF)) ? t2 : t1; }
      TR_STEP(8, XOR8) TR_STEP(4, XOR7) TR_STEP(2, XOR2) TR_STEP(1, XOR1)
#undef TR_STEP
      mx = fmaxf(mx, x[0]);
      sco[(w * NG + g) * 64 + lane] = x[0];
      __builtin_amdgcn_sched_barrier(0);
    };
    auto vstep = [&](auto I) {
      constexpr int i = I.value;
      asm volatile("" : "+v"(R[i]));
      const float pu = dpp<0x150 + i>(pr);
#pragma unroll
      for (int k = 0; k < 4; ++k) { acc[2 * k] = fmaf(pu, blo(R[i][k]), acc[2 * k]); acc[2 * k + 1] = fmaf(pu, bhi(R[i][k]), acc[2 * k + 1]); }
      asm volatile("" : "+v"(acc[0]), "+v"(acc[1]), "+v"(acc[2]), "+v"(acc[3]), "+v"(acc[4]), "+v"(acc[5]), "+v"(acc[6]), "+v"(acc[7]));
    };
    auto request = [&](auto I, int n0) {
      constexpr int t = I.value + DEPTH;
      const int n = n0 + (t >> 3);
      if (t % 4 == 0) pace(n);
      R[t & 15] = BLOAD(rdesc(n), vo, roff(n) + (t & 7) * 4096);
      __builtin_amdgcn_sched_barrier(0);
    };
    auto wsync = [&]() {
      if (WSYNC) {
        unsigned* sl = cnt + 4;
        if (lane == 0) __hip_atomic_store(sl + w, slip, __ATOMIC_RELAXED, __HIP_MEMORY_SCOPE_WORKGROUP);
        unsigned o = __hip_atomic_load(sl + (lane & 3), __ATOMIC_RELAXED, __HIP_MEMORY_SCOPE_WORKGROUP);
        const unsigned m = ((unsigned)__builtin_amdgcn_readlane(o, 0) + (unsigned)__builtin_amdgcn_readlane(o, 1) + (unsigned)__builtin_amdgcn_readlane(o, 2) + (unsigned)__builtin_amdgcn_readlane(o, 3)) >> 2;
        due += m - slip; slip = m;
        __builtin_amdgcn_sched_barrier(0);
      }
    };
    lds_await(cnt, 1);
    q = *reinterpret_cast<const u32x4*>(qbs + c * 4);
    if (w == 0) STAMP(1);
    mx = -INFINITY;
#pragma unroll
    for (int i = 0; i < 16; ++i) {
      kdot(R[i], i);
      if (i < DEPTH) { if (i % 4 == 0) pace(0); R[i] = BLOAD(rdesc(0), vo, roff(2 + 2 * NE + (i >> 3)) + (i & 7) * 4096); }
      __builtin_amdgcn_sched_barrier(0);
    }
    kfinish(0);
#pragma unroll
    for (int e = 0; e < NE; ++e) {
#pragma unroll
      for (int i = 0; i < 16; ++i) { kdot(E[e][i], i); __builtin_amdgcn_sched_barrier(0); }
      kfinish(1 + e);
    }
#pragma unroll 1
    for (int br = 0; br < 2; ++br) {
#pragma unroll 1
      for (int g = br ? 0 : 1 + NE; g < NG; ++g) {
        const int n0 = br * 32 + g * 2;
#define KSTEP(i) kdot(R[i], i); request(IC<i>{}, n0);
        wsync();
        FOR16(KSTEP)
#undef KSTEP
        TR(br * 16 + g);
        kfinish(g);
      }
      const float mw = xrow_max(rowmax16(mx)), msc = mw * SC;
      l = 0.f;
      dstep += (unsigned)((br ? DP3 : DP1) / 2);
#pragma unroll
      for (int k = 0; k < 8; ++k) acc[k] = 0.f;
#pragma unroll 1
      for (int g = 0; g < NG; ++g) {
        const int n0 = br * 32 + 16 + g * 2;
        pr = __builtin_amdgcn_exp2f(fmaf(sco[(w * NG + g) * 64 + lane], SC, -msc));
        l += pr;
#define VSTEP(i) vstep(IC<i>{}); request(IC<i>{}, n0);
        wsync();
        FOR16(VSTEP)
#undef VSTEP
        TR(br * 16 + 8 + g);
      }
      if (w == 0 && br == 1) STAMP(2);
      *reinterpret_cast<f32x2*>(ow + (br * SW + w) * DH + c * 8 + r * 2) = rows_reduce8(acc);
      const float lw = xrow_sum(rowsum16(l));
      if (lane == 0) { mlw[br * 8 + w] = mw; mlw[br * 8 + SW + w] = lw; }
      wg_barrier();
      dstep += (unsigned)(DP2 / 2);
      q = *reinterpret_cast<const u32x4*>(qbs + 64 + c * 4);
      mx = -INFINITY;
    }
    STFLUSH();
  } else {
    const int ww = w - SW, br = ww >> 1, bb = ww & 1, g = lane >> 3, cc = lane & 7;
    gu64* const ws = (gu64*)p.ws;
    uint16_t* outp = p.out;
    const uint16_t *pxa = p.x[0], *pxb = p.x[1], *pqa = p.wq[0], *pqb = p.wq[1], *poa = p.wo[0], *pob = p.wo[1];
    asm volatile("" : "+s"(outp), "+s"(pxa), "+s"(pxb), "+s"(pqa), "+s"(pqb), "+s"(poa), "+s"(pob));
    const unsigned tbeg = now32();
    auto qdot = [&](float (&qa)[8], const u32x4& w0, const u32x4& w1, unsigned xw) {
#pragma unroll
      for (int k = 0; k < 4; ++k) {
        qa[2 * k] = dot2(__builtin_amdgcn_perm(w1[k], w0[k], 0x05040100u), xw, qa[2 * k]);
        qa[2 * k + 1] = dot2(__builtin_amdgcn_perm(w1[k], w0[k], 0x07060302u), xw, qa[2 * k + 1]);
      }
      __builtin_amdgcn_sched_barrier(0);
    };
    unsigned long long eh, er;
    Tag tag, tagy;
    unsigned tpub = 0;
    auto collect = [&]() {
    if (bb == b) {
      const unsigned tq = br ? tbeg + QB_WAIT : (QD ? tpub + QD : tbeg + QA_WAIT);
      while ((int)(now32() - tq) < 0) __builtin_amdgcn_s_sleep(2);
      gu64* src = ws + QX_OFF + ((br * NH + h) * 2 + bb) * 8 * DH + lane;
      unsigned v[16];
      const bool got = sweep<16, QGAP>([&](int i) { return src + i * 64; }, tag, v);
      float s0 = got ? 0.f : __builtin_nanf(""), s1 = 0.f;
#pragma unroll
      for (int s = 0; s < 8; ++s) { s0 += __uint_as_float(v[2 * s]); s1 += __uint_as_float(v[2 * s + 1]); }
      qbs[br * 64 + 4 * c + r] = pack_bf16(s0, s1);
      if (!br) { lds_arrive(cnt, lane); STAMP(11); }
    }
    };
    {
      const int row = 64 * s8 + 32 * bb + 8 * r;
      const auto rx = RSRC(br ? pxb : pxa, 2 * DM * 2);
      const auto rq = RSRC((br ? pqb : pqa) + h * DH, DM * WQ_ROW - h * DH * 2);
      const auto ro = RSRC((bb ? pob : poa) + (size_t)h * DH * DM, DH * DM * 2);
      typedef __attribute__((address_space(3))) void* lds_ptr;
      u32x4 W[8], xv[2];
#pragma unroll
      for (int i = 0; i < 8; ++i) { W[i] = BLOAD(rq, row * WQ_ROW + c * 16, i * WQ_ROW); __builtin_amdgcn_sched_barrier(0); }
#pragma unroll
      for (int t = 0; t < 2; ++t) xv[t] = __builtin_amdgcn_raw_buffer_load_b128(rx, row * 2, t * DM * 2, 0);
      eh = __hip_atomic_load(ws + EH_OFF + h * 32, __ATOMIC_RELAXED, __HIP_MEMORY_SCOPE_AGENT);
      er = __hip_atomic_load(ws + ER_OFF + s8 * 32, __ATOMIC_RELAXED, __HIP_MEMORY_SCOPE_AGENT);
      __builtin_amdgcn_sched_barrier(0);
      __builtin_amdgcn_s_barrier();
      __builtin_amdgcn_sched_barrier(0);
      if (br) {
#pragma unroll
        for (int n = 0; n < 16; ++n)
          __builtin_amdgcn_raw_ptr_buffer_load_lds(ro, (lds_ptr)(wl + (16 * bb + n) * 64), 16, g * (DM * 2) + s8 * 128 + cc * 16, n * 8 * (DM * 2), 0, 2);
        __builtin_amdgcn_sched_barrier(0);
        if (ww == 2) STAMP(12);
        __builtin_amdgcn_s_barrier();
        __builtin_amdgcn_sched_barrier(0);
      }
#pragma unroll
      for (int t = 0; t < 2; ++t) {
        float qa[8];
#pragma unroll
        for (int i = 0; i < 8; ++i) qa[i] = 0.f;
#pragma unroll
        for (int i = 0; i < 4; ++i) qdot(qa, W[2 * i], W[2 * i + 1], xv[t][i]);
        *reinterpret_cast<f32x2*>(qp + (ww * 2 + t) * DH + lane * 2) = rows_reduce8(qa);
      }
      lds_arrive(cnt + 8 + br, lane);
      eh += 1;
      tag.lo = __builtin_amdgcn_readfirstlane((unsigned)eh); tag.hi = __builtin_amdgcn_readfirstlane((unsigned)(eh >> 32)) & 0xffu;
      lds_await(cnt + 8 + br, 2);
      {
        const f32x2 t = *reinterpret_cast<const f32x2*>(qp + (br * 4 + bb) * DH + lane * 2) + *reinterpret_cast<const f32x2*>(qp + (br * 4 + 2 + bb) * DH + lane * 2);
        gu64* dst = ws + QX_OFF + (((br * NH + h) * 2 + bb) * 8 + s8) * DH + lane;
        if (br) { asm volatile("s_waitcnt vmcnt(0)" ::: "memory"); lds_arrive(cnt + 10, lane); }
        put(dst, t[0], tag); put(dst + 64, t[1], tag);
        if (ww == 0) STAMP(10);
        tpub = now32();
      }
      if (!br) { __builtin_amdgcn_sched_barrier(0); __builtin_amdgcn_s_barrier(); __builtin_amdgcn_sched_barrier(0); }
      er += 1;
      tagy.lo = __builtin_amdgcn_readfirstlane((unsigned)er); tagy.hi = __builtin_amdgcn_readfirstlane((unsigned)(er >> 32)) & 0xffu;
      asm volatile("" : "+s"(tagy.lo), "+s"(tagy.hi));
    }
    collect();
    lds_await(cnt + 10, 2);
    u32x4 Wo[16];
#pragma unroll
    for (int k = 0; k < 8; ++k) {
      const u32x4 w0 = wl[(16 * br + 2 * k) * 64 + lane], w1 = wl[(16 * br + 2 * k + 1) * 64 + lane];
#pragma unroll
      for (int i = 0; i < 4; ++i) { Wo[2 * k][i] = __builtin_amdgcn_perm(w1[i], w0[i], 0x05040100u); Wo[2 * k + 1][i] = __builtin_amdgcn_perm(w1[i], w0[i], 0x07060302u); }
      asm volatile("" : "+v"(Wo[2 * k]), "+v"(Wo[2 * k + 1]));
    }
    gu64* const ox = ws + OX_OFF + (br * NH + h) * 8 * OX_STR;
    if (SWARM) {
      auto touch8 = [](const unsigned long long* a) {
        unsigned t0, t1, t2, t3, t4, t5, t6, t7;
        asm volatile("s_load_dword %0, %8, 0x0\n\ts_load_dword %1, %8, 0x40\n\ts_load_dword %2, %8, 0x80\n\ts_load_dword %3, %8, 0xc0\n\t"
                     "s_load_dword %4, %8, 0x100\n\ts_load_dword %5, %8, 0x140\n\ts_load_dword %6, %8, 0x180\n\ts_load_dword %7, %8, 0x1c0\n\ts_waitcnt lgkmcnt(0)"
                     : "=&s"(t0), "=&s"(t1), "=&s"(t2), "=&s"(t3), "=&s"(t4), "=&s"(t5), "=&s"(t6), "=&s"(t7) : "s"(a) : "memory");
      };
      while ((int)(now32() - (tbeg + SWARM)) < 0) __builtin_amdgcn_s_sleep(32);
      touch8(p.ws + YX_OFF + ((br * 2 + bb) * 8 + s8) * (NH * 64) + h * 64);
      touch8(p.ws + OX_OFF + ((NH + h) * 8 + s8) * OX_STR + (ww < 2 ? ww * 64 : 80));
    }
    gu64* const yx = ws + YX_OFF + ((br * 2 + bb) * 8 + s8) * (NH * 64);
    gu64* osrc = ox + 4 * bb * OX_STR + lane;
    gu64* msrc = ox + (4 * bb + (lane & 3)) * OX_STR + 128;
    gu64* ydst = yx + h * 64 + 8 * cc + 2 * r + ((lane >> 3) & 1);
    gu64* ysrc = yx + g * 64 + 2 * h + cc;
    asm volatile("" : "+v"(osrc), "+v"(msrc), "+v"(ydst), "+v"(ysrc));
#pragma unroll 1
    for (int it = 0; it < 2; ++it) {
      wg_barrier();
      if (it == 1 && ww == 2) STAMP(3);
      if (it == br && bb == b) {
        const f32x4 mv = *reinterpret_cast<const f32x4*>(mlw + br * 8), lv = *reinterpret_cast<const f32x4*>(mlw + br * 8 + SW);
        f32x2 o[SW];
#pragma unroll
        for (int i = 0; i < SW; ++i) { const float* s = ow + (br * SW + i) * DH + 16 * cc + g; o[i] = f32x2{s[0], s[8]}; }
        const float M = fmaxf(fmaxf(mv[0], mv[1]), fmaxf(mv[2], mv[3]));
        float L = 0.f, s0 = 0.f, s1 = 0.f;
#pragma unroll
        for (int i = 0; i < SW; ++i) { const float f = __builtin_amdgcn_exp2f((mv[i] - M) * SC); L = fmaf(f, lv[i], L); s0 = fmaf(f, o[i][0], s0); s1 = fmaf(f, o[i][1], s1); }
        gu64* dst = ox + s8 * OX_STR + lane;
        put<PUTX & 2>(dst, s0, tag); put<PUTX & 2>(dst + 64, s1, tag);
        if (lane < 3) put<PUTX & 2>(dst + 128, lane == 0 ? L : __uint_as_float(lane == 1 ? __float_as_uint(M) & 0xffffff00u : (__float_as_uint(M) & 0xffu) << 8), tag);
      }
    }
    if (br) __builtin_amdgcn_s_sleep(G1);
    float y[8];
#pragma unroll
    for (int i = 0; i < 8; ++i) { y[i] = 0.f; asm volatile("" : "+v"(y[i])); }
    unsigned v[11];
    bool got = sweep<11, PG1>([&](int i) { return i < 8 ? osrc + i * 64 + (i >> 1) * (OX_STR - 128) : msrc + (i - 8); }, tag, v);
    if (ww == 2) STAMP(4);
    const float mq = __uint_as_float(v[9] | (v[10] >> 8));
    float M = fmaxf(mq, dpp<XOR1>(mq)); M = fmaxf(M, dpp<XOR2>(M));
    const float e = __builtin_amdgcn_exp2f((mq - M) * SC);
    float den = e * __uint_as_float(v[8]); den += dpp<XOR1>(den); den += dpp<XOR2>(den);
    const float cw = e * __builtin_amdgcn_rcpf(den);
    const float c0 = dpp<0x00>(cw), c1 = dpp<0x55>(cw), c2 = dpp<0xAA>(cw), c3 = dpp<0xFF>(cw);
    const float s0 = fmaf(c3, __uint_as_float(v[6]), fmaf(c2, __uint_as_float(v[4]), fmaf(c1, __uint_as_float(v[2]), c0 * __uint_as_float(v[0]))));
    const float s1 = fmaf(c3, __uint_as_float(v[7]), fmaf(c2, __uint_as_float(v[5]), fmaf(c1, __uint_as_float(v[3]), c0 * __uint_as_float(v[1]))));
    const int op = (int)pack_bf16(s0, s1);
    int ok[8];
#define OB(k) ok[k] = __builtin_amdgcn_mov_dpp(op, 0x150 + k, 0xf, 0xf, true);
    OB(0) OB(1) OB(2) OB(3) OB(4) OB(5) OB(6) OB(7)
#undef OB
#define OB(k) ok[k] = __builtin_amdgcn_update_dpp(ok[k], op, 0x158 + k, 0xf, 0xc, false);
    OB(0) OB(1) OB(2) OB(3) OB(4) OB(5) OB(6) OB(7)
#undef OB
    asm volatile("" : "+v"(ok[0]), "+v"(ok[1]), "+v"(ok[2]), "+v"(ok[3]), "+v"(ok[4]), "+v"(ok[5]), "+v"(ok[6]), "+v"(ok[7]));
#pragma unroll
    for (int k = 0; k < 8; ++k)
#pragma unroll
      for (int i = 0; i < 8; ++i) y[i] = dot2(Wo[2 * k + (i & 1)][i >> 1], (unsigned)ok[k], y[i]);
    const f32x2 t = rows_reduce8(y);
    float yc = ((lane & 8) ? t[1] : t[0]) + dpp<XOR8>((lane & 8) ? t[0] : t[1]);
    if (!got) yc = __builtin_nanf("");
    put<PUTX & 2>(ydst, yc, tagy);
    if (ww == 2) STAMP(5);
    if (br == 0 && s8 == 0 && lane == 0) {
      if (__hip_atomic_fetch_add(cnt + 2, 1u, __ATOMIC_RELAXED, __HIP_MEMORY_SCOPE_WORKGROUP) == 1u) __hip_atomic_store(ws + EH_OFF + h * 32, eh, __ATOMIC_RELAXED, __HIP_MEMORY_SCOPE_AGENT);
    }
    if ((h & 3) == 0) {
      if (br) __builtin_amdgcn_s_sleep(G2); else __builtin_amdgcn_s_sleep(GA);
      unsigned zb[4];
      got = sweep<4, PG2>([&](int i) { return ysrc + i * 8 * 64; }, tagy, zb);
      float z = (__uint_as_float(zb[0]) + __uint_as_float(zb[1])) + (__uint_as_float(zb[2]) + __uint_as_float(zb[3]));
      z += dpp<XOR8>(z);
      z = xrow_sum(z);
      if (!got) z = __builtin_nanf("");
      const unsigned o2 = pack_bf16(z, dpp<XOR1>(z));
      if (lane < 8 && !(lane & 1)) *reinterpret_cast<unsigned*>(outp + bb * (2 * DM) + br * DM + s8 * 64 + 2 * h + lane) = o2;
      if (h == 0 && ww == 0 && lane == 0) __hip_atomic_store(ws + ER_OFF + s8 * 32, er, __ATOMIC_RELAXED, __HIP_MEMORY_SCOPE_AGENT);
    }
    STFLUSH();
  }
}

extern "C" void kernel_launch(void* const* d_in, const int* in_sizes, int n_in, void* d_out, int out_size, void* d_ws, size_t ws_size,
                              hipStream_t stream) {
  Params p;
  for (int i = 0; i < 2; ++i) {
    p.x[i] = (const uint16_t*)d_in[5 * i + 0];
    p.kv[2 * i] = (const uint16_t*)d_in[5 * i + 1];
    p.kv[2 * i + 1] = (const uint16_t*)d_in[5 * i + 2];
    p.wq[i] = (const uint16_t*)d_in[5 * i + 3];
    p.wo[i] = (const uint16_t*)d_in[5 * i + 4];
  }
#define WSEND 0
  p.ws = (unsigned long long*)((char*)d_ws + (WSEND ? ((ws_size - ((size_t)WSEND << 20)) & ~(size_t)0xfffff) : 0));
  p.out = (uint16_t*)d_out;
  attn_main<<<dim3(256), dim3(512), 0, stream>>>(p);
}
```
